```python
import torch

def _softmax(x, axis=-1):
    return torch.softmax(x, dim=axis)
def _gelu(x):
    return torch.nn.functional.gelu(x, approximate="tanh")
def _silu(x):
    return torch.nn.functional.silu(x)
def _sigmoid(x):
    return torch.sigmoid(x)
def _layernorm(x, axis=-1, eps=1e-5):
    m = x.mean(dim=axis, keepdim=True)
    v = x.var(dim=axis, keepdim=True, unbiased=False)
    return (x - m) / torch.sqrt(v + eps)
def _scatter_add(tgt, idx, src, axis=0):
    return tgt.clone().index_add_(axis, idx, src)
def _segment_sum(src, seg, n):
    out = torch.zeros((n,) + src.shape[1:], dtype=src.dtype)
    return out.index_add_(0, seg, src)
def _pad(x, pw):
    flat = tuple(v for pair in reversed(pw) for v in pair)
    return torch.nn.functional.pad(x, flat)

kernel_name = 'parallel_sum__mha_block_cached__geglu_ffn__decode'

def setup_inputs():

    _w = (lambda a: (lambda t: t.to(torch.bfloat16) if t.is_floating_point() else t)(torch.as_tensor(a)))
    def _A_setup():
        B, Sq, D = 1, 1, 576
        H, Sk, Dh = 32, 16384, 128
        return {
            "x": _w((lambda *s: torch.randn(*s, dtype=torch.bfloat16) * 0.05)(B, Sq, D)),
            "k_cache": _w((lambda *s: torch.randn(*s, dtype=torch.bfloat16) * 3.2)(B, H, Sk, Dh)),
            "v_cache": _w((lambda *s: torch.randn(*s, dtype=torch.bfloat16) * 1.6)(B, H, Sk, Dh)),
            "wq": _w((lambda *s: torch.randn(*s, dtype=torch.bfloat16) * 3.2)(D, H * Dh) * (D ** -0.5)),
            "wo": _w((lambda *s: torch.randn(*s, dtype=torch.bfloat16) * 1.6)(H * Dh, D) * ((H * Dh) ** -0.5)),
        }
    _A_in = _A_setup()

    def _B_setup():
        B, S, D, F = 1, 1, 576, 4608
        return {
            "x": _w((lambda *s: torch.randn(*s, dtype=torch.bfloat16) * 0.05)(B, S, D)),
            "w_gate": _w((lambda *s: torch.randn(*s, dtype=torch.bfloat16) * 0.05)(D, F)),
            "w_up": _w((lambda *s: torch.randn(*s, dtype=torch.bfloat16) * 0.05)(D, F)),
            "w_down": _w((lambda *s: torch.randn(*s, dtype=torch.bfloat16) * 0.05)(F, D)),
        }
    _B_in = _B_setup()

    return {
        "A_x": _A_in["x"],
        "A_k_cache": _A_in["k_cache"],
        "A_v_cache": _A_in["v_cache"],
        "A_wq": _A_in["wq"],
        "A_wo": _A_in["wo"],
        "B_x": _B_in["x"],
        "B_w_gate": _B_in["w_gate"],
        "B_w_up": _B_in["w_up"],
        "B_w_down": _B_in["w_down"],
    }

def reference(A_x, A_k_cache, A_v_cache, A_wq, A_wo, B_x, B_w_gate, B_w_up, B_w_down):
    def _A_ref(x, k_cache, v_cache, wq, wo):
        B, Sq, D = x.shape
        H, Dh = 32, 128
        q = (lambda x, *d: x.permute(*d))((x @ wq).reshape(B, Sq, H, Dh), 0, 2, 1, 3)
        scores = torch.einsum("bhqd,bhkd->bhqk", q, k_cache) * (Dh ** -0.5)
        p = _softmax(scores, axis=-1)
        o = torch.einsum("bhqk,bhkd->bhqd", p, v_cache)
        return (lambda x, *d: x.permute(*d))(o, 0, 2, 1, 3).reshape(B, Sq, H * Dh) @ wo

    def _B_ref(x, w_gate, w_up, w_down):
        return (_gelu(x @ w_gate) * (x @ w_up)) @ w_down

    x = A_x
    xn = _layernorm(x, axis=-1)
    return x + _A_ref(xn, A_k_cache, A_v_cache, A_wq, A_wo) + _B_ref(xn, B_w_gate, B_w_up, B_w_down)
```

```cpp
#include <hip/hip_runtime.h>
#include <hip/hip_bf16.h>
#include <stdint.h>

typedef unsigned u32x4 __attribute__((ext_vector_type(4)));
typedef float f32x4 __attribute__((ext_vector_type(4)));
typedef __bf16 bf16x2 __attribute__((ext_vector_type(2)));

namespace {

constexpr int DM = 576, NH = 32, SK = 16384, DH = 128, FF = 4608;
constexpr int NWG = 256, NTHR = 512, NWAVE = 8;
constexpr int NTHR_ALL = NTHR + 64;
constexpr int NSPLIT = 8;
constexpr int NCHUNK = SK / 32;
constexpr int NST = 6;
constexpr int DYN0 = NST * NSPLIT * NWAVE;
constexpr int NDYN = 2 * (NCHUNK - DYN0);
constexpr unsigned TQ_EMPTY = 0xFFFFFFFFu;
constexpr int WIN = 4;
constexpr int PRE = 2;
constexpr int PRE1 = 6;
constexpr int PACE = 6;
constexpr int FH = FF / NH;
constexpr int FCOL = FH / NSPLIT;
constexpr int P1 = DH + 2 * FH;
constexpr int NRED = 72;
constexpr float LOG2E = 1.4426950408889634f;
constexpr float SCALE = 0.08838834764831845f;

constexpr unsigned WS_EPOCH = 0;
constexpr unsigned WS_CTR = 8192;
constexpr unsigned WS_P1 = WS_CTR + NH * 256;
constexpr unsigned P1_BYTES = (P1 / 2) * 16;
constexpr unsigned O_BYTES = 130 * 16 + 32;
constexpr unsigned WS_O = WS_P1 + NWG * P1_BYTES;
constexpr unsigned WS_YA = WS_O + NWG * O_BYTES;
constexpr unsigned WS_YF = WS_YA + NRED * NWG * 64;
constexpr unsigned WS_END = WS_YF + NRED * NWG * 64;

constexpr int L_XS = 0;
constexpr int L_QP = L_XS + NWAVE * DM;
constexpr int L_FP = L_QP + 64;
constexpr int L_GU = L_FP + NWAVE * P1;
constexpr int L_YS = L_GU + NSPLIT * 36;
constexpr int L_YP = L_YS + DM;
constexpr int L_OV = L_YP + NWAVE * DM;
constexpr int L_MG = L_OV + 16;
constexpr int L_RD = L_MG + NWAVE * 260;
constexpr int L_TAG = L_RD + NWAVE * 16;
constexpr int L_REQ = L_TAG + 4;
constexpr int L_TQ = L_REQ + 4;
constexpr int L_END = L_TQ + ((NDYN + NWAVE + 3) & ~3);

__device__ __forceinline__ float bf_lo(unsigned u) { return __uint_as_float(u << 16); }
__device__ __forceinline__ float bf_hi(unsigned u) { return __uint_as_float(u & 0xffff0000u); }
__device__ __forceinline__ float rbf(float x) { return (float)(__bf16)x; }
__device__ __forceinline__ unsigned pack_bf(float lo, float hi) {
  bf16x2 v; v[0] = (__bf16)lo; v[1] = (__bf16)hi;
  return __builtin_bit_cast(unsigned, v);
}
__device__ __forceinline__ float dot2(unsigned a, unsigned b, float c) {
  return __builtin_amdgcn_fdot2_f32_bf16(__builtin_bit_cast(bf16x2, a), __builtin_bit_cast(bf16x2, b), c, false);
}
template <int CTRL> __device__ __forceinline__ float dpp(float x) {
  return __builtin_bit_cast(float, __builtin_amdgcn_mov_dpp(__builtin_bit_cast(int, x), CTRL, 0xf, 0xf, true));
}
constexpr int XOR1 = 0xB1, XOR2 = 0x4E, XOR7 = 0x141, ROR8 = 0x128, ROR4 = 0x124, ROR2 = 0x122, ROR1 = 0x121;
__device__ __forceinline__ float x16_sum(float x) {
  auto s = __builtin_amdgcn_permlane16_swap(__float_as_uint(x), __float_as_uint(x), false, false);
  return __uint_as_float(s[0]) + __uint_as_float(s[1]);
}
__device__ __forceinline__ float x32_sum(float x) {
  auto s = __builtin_amdgcn_permlane32_swap(__float_as_uint(x), __float_as_uint(x), false, false);
  return __uint_as_float(s[0]) + __uint_as_float(s[1]);
}
__device__ __forceinline__ float x16_max(float x) {
  auto s = __builtin_amdgcn_permlane16_swap(__float_as_uint(x), __float_as_uint(x), false, false);
  return fmaxf(__uint_as_float(s[0]), __uint_as_float(s[1]));
}
__device__ __forceinline__ float x32_max(float x) {
  auto s = __builtin_amdgcn_permlane32_swap(__float_as_uint(x), __float_as_uint(x), false, false);
  return fmaxf(__uint_as_float(s[0]), __uint_as_float(s[1]));
}
__device__ __forceinline__ double mkd(unsigned lo, unsigned hi) { return __builtin_bit_cast(double, ((unsigned long long)hi << 32) | lo); }
__device__ __forceinline__ unsigned dlo(double x) { return (unsigned)__builtin_bit_cast(unsigned long long, x); }
__device__ __forceinline__ unsigned dhi(double x) { return (unsigned)(__builtin_bit_cast(unsigned long long, x) >> 32); }
template <int CTRL> __device__ __forceinline__ double dppd(double x) {
  return mkd((unsigned)__builtin_amdgcn_mov_dpp((int)dlo(x), CTRL, 0xf, 0xf, true), (unsigned)__builtin_amdgcn_mov_dpp((int)dhi(x), CTRL, 0xf, 0xf, true));
}
__device__ __forceinline__ double x16_sumd(double x) {
  auto a = __builtin_amdgcn_permlane16_swap(dlo(x), dlo(x), false, false);
  auto b = __builtin_amdgcn_permlane16_swap(dhi(x), dhi(x), false, false);
  return mkd(a[0], b[0]) + mkd(a[1], b[1]);
}
__device__ __forceinline__ double x32_sumd(double x) {
  auto a = __builtin_amdgcn_permlane32_swap(dlo(x), dlo(x), false, false);
  auto b = __builtin_amdgcn_permlane32_swap(dhi(x), dhi(x), false, false);
  return mkd(a[0], b[0]) + mkd(a[1], b[1]);
}
__device__ __forceinline__ double pow2d(float a, float b) { return __builtin_ldexp(1.0, (int)fmaxf(a - b, -2000.f)); }
__device__ __forceinline__ float row_sum(float x) {
  x += dpp<ROR8>(x); x += dpp<ROR4>(x); x += dpp<ROR2>(x); x += dpp<ROR1>(x); return x;
}
__device__ __forceinline__ float wave_sum(float x) { return x32_sum(x16_sum(row_sum(x))); }

__device__ __forceinline__ u32x4 ld16(const void* p) { return *reinterpret_cast<const u32x4*>(p); }
__device__ __forceinline__ u32x4 ld16_nt(const void* p) { return __builtin_nontemporal_load(reinterpret_cast<const u32x4*>(p)); }
template <int I> __device__ __forceinline__ float bcast16(float p) { return dpp<0x150 + I>(p); }

__device__ __forceinline__ void fma8(float (&a)[8], float s, u32x4 w) {
#pragma unroll
  for (int k = 0; k < 4; ++k) { a[2 * k] = fmaf(s, bf_lo(w[k]), a[2 * k]); a[2 * k + 1] = fmaf(s, bf_hi(w[k]), a[2 * k + 1]); }
}

__device__ __forceinline__ float gelu_tanh(float x) {
  const float k0 = 0.7978845608028654f, k1 = 0.044715f;
  const float inner = k0 * (x + k1 * x * x * x);
  return 0.5f * x * (1.0f + tanhf(inner));
}

constexpr int AUX_SC1 = 16;
__device__ __forceinline__ void st_gran(__amdgpu_buffer_rsrc_t rs, unsigned off, unsigned tag, float a, float b) {
  u32x4 v = {tag, __float_as_uint(a), tag, __float_as_uint(b)};
  __builtin_amdgcn_raw_buffer_store_b128(v, rs, (int)off, 0, AUX_SC1);
}
__device__ __forceinline__ void st_gran_asm(unsigned char* p, unsigned tag, unsigned a, unsigned b) {
  u32x4 v = {tag, a, tag, b};
  asm volatile("global_store_dwordx4 %0, %1, off sc1\n\ts_nop 1" :: "v"(p), "v"(v) : "memory");
}
__device__ __forceinline__ void st_grand(__amdgpu_buffer_rsrc_t rs, unsigned off, unsigned tag, double a) {
  u32x4 v = {tag, dlo(a), tag, dhi(a)};
  __builtin_amdgcn_raw_buffer_store_b128(v, rs, (int)off, 0, AUX_SC1);
}
__device__ __forceinline__ u32x4 ld_gran(__amdgpu_buffer_rsrc_t rs, unsigned off) {
  return __builtin_amdgcn_raw_buffer_load_b128(rs, (int)off, 0, AUX_SC1);
}

template <int NROW>
__device__ __forceinline__ void gemv_publish(float* lds, __amdgpu_buffer_rsrc_t rs, unsigned yb, unsigned tag, float vec,
                                             const u32x4 (&W)[21], int lane) {
  float y[8], y2[8];
#pragma unroll
  for (int k = 0; k < 8; ++k) { y[k] = 0.f; y2[k] = 0.f; }
  constexpr int NX = (NROW + 7) / 8;
  float mx[NX];
#pragma unroll
  for (int i = 0; i < NX; ++i) {
    const int row = 8 * i + (lane >> 3);
    const float t = __shfl(vec, row < NROW ? row : 0);
    mx[i] = row < NROW ? t : 0.f;
  }
#pragma unroll
  for (int i = 0; i < NROW; ++i) {
    u32x4 w = W[i];
    asm volatile("" : "+v"(w));
    fma8(y, __uint_as_float(__builtin_amdgcn_readlane(__float_as_uint(vec), i)), w);
    asm volatile("" : "+v"(y[0]), "+v"(y[1]), "+v"(y[2]), "+v"(y[3]), "+v"(y[4]), "+v"(y[5]), "+v"(y[6]), "+v"(y[7]));
  }
#pragma unroll
  for (int i = 0; i < NX; ++i) {
    u32x4 w = W[18 + i];
    asm volatile("" : "+v"(w));
    fma8(y2, mx[i], w);
    asm volatile("" : "+v"(y2[0]), "+v"(y2[1]), "+v"(y2[2]), "+v"(y2[3]), "+v"(y2[4]), "+v"(y2[5]), "+v"(y2[6]), "+v"(y2[7]));
  }
#pragma unroll
  for (int k = 0; k < 8; ++k) { float t = y2[k]; t += dpp<ROR8>(t); y2[k] = x32_sum(x16_sum(t)); }
  float* ys = lds + L_YS;
  *reinterpret_cast<f32x4*>(ys + lane * 8) = f32x4{y[0], y[1], y[2], y[3]};
  *reinterpret_cast<f32x4*>(ys + lane * 8 + 4) = f32x4{y[4], y[5], y[6], y[7]};
  if (lane < 8) {
    *reinterpret_cast<f32x4*>(ys + 512 + lane * 8) = f32x4{y2[0], y2[1], y2[2], y2[3]};
    *reinterpret_cast<f32x4*>(ys + 512 + lane * 8 + 4) = f32x4{y2[4], y2[5], y2[6], y2[7]};
  }
  __builtin_amdgcn_s_waitcnt(0xC07F);
  __builtin_amdgcn_wave_barrier();
#pragma unroll
  for (int k = 0; k < 5; ++k) {
    const int t = lane + 64 * k;
    if (k < 4 || lane < 32) {
      const float2 v = *reinterpret_cast<const float2*>(ys + 2 * t);
      st_gran(rs, yb + (unsigned)(t >> 2) * (NWG * 64) + (unsigned)(t & 3) * 16, tag, v.x, v.y);
    }
  }
  __builtin_amdgcn_s_waitcnt(0xC07F);
  __builtin_amdgcn_wave_barrier();
}

__device__ __forceinline__ void gather_part(float* rd, __amdgpu_buffer_rsrc_t rs, unsigned tag, unsigned off, int lane, int wave, int nap) {
  u32x4 a0, a1;
  unsigned spins = 0;
  for (;;) {
    asm volatile("" ::: "memory");
    a0 = ld_gran(rs, off); a1 = ld_gran(rs, off + 16);
    if (__all((a0[0] == tag) & (a0[2] == tag) & (a1[0] == tag) & (a1[2] == tag))) break;
    if (++spins > (1u << 18)) break;
    if (nap) __builtin_amdgcn_s_sleep(8); else __builtin_amdgcn_s_sleep(1);
  }
  float v[4] = {__uint_as_float(a0[1]), __uint_as_float(a0[3]), __uint_as_float(a1[1]), __uint_as_float(a1[3])};
#pragma unroll
  for (int k = 0; k < 4; ++k) {
    float t = v[k];
    t += dpp<ROR8>(t); t += dpp<ROR4>(t); t += dpp<ROR2>(t);
    v[k] = x32_sum(x16_sum(t));
  }
  if (lane < 2) *reinterpret_cast<f32x4*>(rd + wave * 8 + lane * 4) = f32x4{v[0], v[1], v[2], v[3]};
}

__global__ void __launch_bounds__(NTHR_ALL, 3)
fused_decode_kernel(const unsigned short* __restrict__ x, const unsigned short* __restrict__ kc,
                    const unsigned short* __restrict__ vc, const unsigned short* __restrict__ wq,
                    const unsigned short* __restrict__ wo, const unsigned short* __restrict__ wg,
                    const unsigned short* __restrict__ wu, const unsigned short* __restrict__ wd,
                    unsigned short* __restrict__ out, unsigned char* __restrict__ ws) {
  static_assert(WIN >= 1 && WIN <= 8, "window");
  __shared__ __attribute__((aligned(16))) float lds[L_END];
  const int tid = threadIdx.x, lane = tid & 63, wave = __builtin_amdgcn_readfirstlane(tid >> 6);
  const int r = lane >> 4, c = lane & 15;
  const int b = blockIdx.x, xcd = b & 7, idx = b >> 3;
  const int head = idx, split = xcd;
  const int prod = head * NSPLIT + split;
  const bool helper = wave == NWAVE;

  const __amdgpu_buffer_rsrc_t rs = __builtin_amdgcn_make_buffer_rsrc(ws, 0, (int)WS_END, 0x00020000);
  unsigned tag;
  float xv = 0.f;

  if (helper) {
    tag = __builtin_amdgcn_raw_buffer_load_b32(rs, (int)WS_EPOCH, 0, AUX_SC1) + 1u;
    if (split == 0) {
      if (lane == 0) __builtin_amdgcn_raw_buffer_store_b32(0u, rs, (int)(WS_CTR + (unsigned)head * 256), 0, AUX_SC1);
      asm volatile("s_waitcnt vmcnt(0)" ::: "memory");
    }
    u32x4 W[21];
    float vec;
    {
      const unsigned short* p = wd + (size_t)(head * FH + split * FCOL) * DM;
#pragma unroll
      for (int i = 0; i < 18; ++i) W[i] = ld16_nt(p + i * DM + lane * 8);
#pragma unroll
      for (int i = 0; i < 3; ++i) { const int row = 8 * i + (lane >> 3); W[18 + i] = ld16_nt(p + (row < FCOL ? row : 0) * DM + 512 + (lane & 7) * 8); }
    }
    if (lane == 0) { reinterpret_cast<unsigned*>(lds)[L_TAG] = tag; reinterpret_cast<unsigned*>(lds)[L_REQ] = 0u; }
    for (int i = lane; i < NDYN + NWAVE; i += 64) reinterpret_cast<unsigned*>(lds)[L_TQ + i] = TQ_EMPTY;
    __syncthreads();
    const unsigned pbase = WS_P1 + (unsigned)head * NSPLIT * P1_BYTES;
    {
      u32x4 qv[NSPLIT];
      unsigned spins = 0;
      for (;;) {
        asm volatile("" ::: "memory");
        bool ok = true;
#pragma unroll
        for (int sp = 0; sp < NSPLIT; ++sp) qv[sp] = ld_gran(rs, pbase + sp * P1_BYTES + (unsigned)lane * 16);
#pragma unroll
        for (int sp = 0; sp < NSPLIT; ++sp) ok &= (qv[sp][0] == tag) & (qv[sp][2] == tag);
        if (__all(ok)) break;
        if (++spins > (1u << 18)) break;
        __builtin_amdgcn_s_sleep(1);
      }
      float q0 = 0.f, q1 = 0.f;
#pragma unroll
      for (int sp = 0; sp < NSPLIT; ++sp) { q0 += __uint_as_float(qv[sp][1]); q1 += __uint_as_float(qv[sp][3]); }
      reinterpret_cast<unsigned*>(lds)[L_QP + lane] = pack_bf(q0, q1);
    }
    __syncthreads();
    {
      u32x4 gv[3];
      unsigned off[3];
#pragma unroll
      for (int k = 0; k < 3; ++k) {
        int n = lane + 64 * k; n = n < 144 ? n : 0;
        const int sp = n / 18, pr = n % 18;
        off[k] = pbase + sp * P1_BYTES + (unsigned)(DH / 2 + (pr / 9) * (FH / 2) + split * (FCOL / 2) + (pr % 9)) * 16;
      }
      unsigned spins = 0;
      for (;;) {
        asm volatile("" ::: "memory");
        bool ok = true;
#pragma unroll
        for (int k = 0; k < 3; ++k) gv[k] = ld_gran(rs, off[k]);
#pragma unroll
        for (int k = 0; k < 3; ++k) ok &= (gv[k][0] == tag) & (gv[k][2] == tag);
        if (__all(ok)) break;
        if (++spins > (1u << 18)) break;
        __builtin_amdgcn_s_sleep(1);
      }
#pragma unroll
      for (int k = 0; k < 3; ++k) {
        const int n = lane + 64 * k;
        if (n < 144) *reinterpret_cast<float2*>(lds + L_GU + 2 * n) = make_float2(__uint_as_float(gv[k][1]), __uint_as_float(gv[k][3]));
      }
    }
    __builtin_amdgcn_s_waitcnt(0xC07F);
    __builtin_amdgcn_wave_barrier();
    const int cl = lane < FCOL ? lane : 0;
    float g = 0.f, u = 0.f;
#pragma unroll
    for (int sp = 0; sp < NSPLIT; ++sp) { g += lds[L_GU + sp * 36 + cl]; u += lds[L_GU + sp * 36 + 18 + cl]; }
    vec = rbf(rbf(gelu_tanh(rbf(g))) * rbf(u));
    gemv_publish<FCOL>(lds, rs, WS_YF + (unsigned)prod * 64, tag, vec, W, lane);
    {
      {
        {
          unsigned served = 0, dead = 0, spins = 0;
          unsigned* ctr = reinterpret_cast<unsigned*>(ws + (WS_CTR + (unsigned)head * 256));
          while (dead < (unsigned)NWAVE) {
            const unsigned req = __builtin_amdgcn_readfirstlane(__hip_atomic_load(reinterpret_cast<unsigned*>(lds) + L_REQ, __ATOMIC_RELAXED, __HIP_MEMORY_SCOPE_WORKGROUP));
            if (req > served) {
              const unsigned n = req - served;
              unsigned base = 0;
              if (lane == 0) base = __hip_atomic_fetch_add(ctr, n, __ATOMIC_RELAXED, __HIP_MEMORY_SCOPE_AGENT);
              base = __builtin_amdgcn_readfirstlane(base);
              unsigned t = base + (unsigned)lane;
              t = t < (unsigned)NDYN ? t : (unsigned)NDYN;
              if ((unsigned)lane < n && served + (unsigned)lane < (unsigned)(NDYN + NWAVE))
                __hip_atomic_store(reinterpret_cast<unsigned*>(lds) + L_TQ + served + lane, t, __ATOMIC_RELAXED, __HIP_MEMORY_SCOPE_WORKGROUP);
              const unsigned live = base < (unsigned)NDYN ? (unsigned)NDYN - base : 0u;
              dead += n - (live < n ? live : n);
              served = req;
              spins = 0;
            } else {
              if (++spins > (1u << 22)) break;
              __builtin_amdgcn_s_sleep(2);
            }
          }
        }
        __syncthreads();
        {
          float ev[NWAVE], E = -INFINITY;
#pragma unroll
          for (int w = 0; w < NWAVE; ++w) { ev[w] = lds[L_MG + w * 260]; E = fmaxf(E, ev[w]); }
          double L = 0.0, o0 = 0.0, o1 = 0.0;
#pragma unroll
          for (int w = 0; w < NWAVE; ++w) {
            const double f = pow2d(ev[w], E);
            const double* rec = reinterpret_cast<const double*>(lds + L_MG + w * 260 + 2);
            L = fma(f, rec[0], L); o0 = fma(f, rec[1 + lane], o0); o1 = fma(f, rec[65 + lane], o1);
          }
          st_grand(rs, WS_O + (unsigned)prod * O_BYTES + (unsigned)lane * 16, tag, o0);
          st_grand(rs, WS_O + (unsigned)prod * O_BYTES + 1024u + (unsigned)lane * 16, tag, o1);
          if (lane < 2) st_grand(rs, WS_O + (unsigned)prod * O_BYTES + 2048u + (unsigned)lane * 16, tag, lane ? (double)E : L);
        }
        {
          const unsigned ob = WS_O + (unsigned)(head * NSPLIT + (lane >> 3)) * O_BYTES;
          u32x4 va, vb, vl, ve;
          unsigned spins = 0;
          for (;;) {
            asm volatile("" ::: "memory");
            va = ld_gran(rs, ob + (unsigned)(16 * split + 2 * (lane & 7)) * 16);
            vb = ld_gran(rs, ob + (unsigned)(16 * split + 2 * (lane & 7) + 1) * 16);
            vl = ld_gran(rs, ob + 2048u);
            ve = ld_gran(rs, ob + 2064u);
            if (__all((va[0] == tag) & (va[2] == tag) & (vb[0] == tag) & (vb[2] == tag) &
                      (vl[0] == tag) & (vl[2] == tag) & (ve[0] == tag) & (ve[2] == tag))) break;
            if (++spins > (1u << 18)) break;
            __builtin_amdgcn_s_sleep(1);
          }
          const float E = (float)mkd(ve[1], ve[3]);
          const float Eh = x32_max(x16_max(fmaxf(E, dpp<ROR8>(E))));
          const double f = pow2d(E, Eh);
          double L = f * mkd(vl[1], vl[3]), o0 = f * mkd(va[1], va[3]), o1 = f * mkd(vb[1], vb[3]);
          L += dppd<ROR8>(L); o0 += dppd<ROR8>(o0); o1 += dppd<ROR8>(o1);
          L = x32_sumd(x16_sumd(L)); o0 = x32_sumd(x16_sumd(o0)); o1 = x32_sumd(x16_sumd(o1));
          if (lane < 8) *reinterpret_cast<float2*>(lds + L_OV + 2 * lane) = make_float2(rbf((float)o0 / (float)L), rbf((float)o1 / (float)L));
        }
      }
      __syncthreads();
    }
    return;
  }
  {
    const u32x4 xa = ld16(x + lane * 8);
    const u32x4 xb = ld16(x + 512 + (lane & 7) * 8);
    u32x4 fw[9];
    const int frow = 72 * split + 9 * wave;
    {
      const int ln = lane < 52 ? lane : 51;
      const unsigned short* p;
      size_t st;
      if (ln < 16) { p = wq + (size_t)frow * (NH * DH) + head * DH + 8 * ln; st = NH * DH; }
      else if (ln < 34) { p = wg + (size_t)frow * FF + head * FH + 8 * (ln - 16); st = FF; }
      else { p = wu + (size_t)frow * FF + head * FH + 8 * (ln - 34); st = FF; }
#pragma unroll
      for (int i = 0; i < 9; ++i) fw[i] = ld16_nt(p + i * st);
    }
    const size_t hoff = (size_t)head * SK * DH + lane * 8;
    const unsigned chunk0 = (unsigned)(split * (NCHUNK / NSPLIT) + wave);
    const unsigned short* kp = kc + hoff + (size_t)chunk0 * (32 * DH);
    const unsigned short* vp = vc + hoff + (size_t)chunk0 * (32 * DH);
    u32x4 S[16];
    __builtin_amdgcn_sched_barrier(0);
#pragma unroll
    for (int i = 0; i < 8; ++i) if (i < PRE) S[i] = ld16_nt(kp + i * 512);
#pragma unroll
    for (int i = 0; i < 8; ++i) if (8 + i < PRE) S[8 + i] = ld16_nt(vp + i * 512);
    __builtin_amdgcn_sched_barrier(0);

    float* xs = lds + L_XS + wave * DM;
    {
      float e[8], f[8];
#pragma unroll
      for (int k = 0; k < 4; ++k) { e[2 * k] = bf_lo(xa[k]); e[2 * k + 1] = bf_hi(xa[k]); f[2 * k] = bf_lo(xb[k]); f[2 * k + 1] = bf_hi(xb[k]); }
      float s = 0.f;
#pragma unroll
      for (int k = 0; k < 8; ++k) s += e[k] + (lane < 8 ? f[k] : 0.f);
      const float mean = wave_sum(s) * (1.0f / DM);
      float q2 = 0.f;
#pragma unroll
      for (int k = 0; k < 8; ++k) { const float d = e[k] - mean; q2 = fmaf(d, d, q2); }
      if (lane < 8) {
#pragma unroll
        for (int k = 0; k < 8; ++k) { const float d = f[k] - mean; q2 = fmaf(d, d, q2); }
      }
      const float var = wave_sum(q2) * (1.0f / DM);
      const float mb = rbf(mean);
      const float den = rbf(sqrtf(rbf(rbf(var) + 1e-5f)));
      f32x4 o0, o1;
#pragma unroll
      for (int k = 0; k < 4; ++k) { o0[k] = rbf(rbf(e[k] - mb) / den); o1[k] = rbf(rbf(e[4 + k] - mb) / den); }
      *reinterpret_cast<f32x4*>(xs + lane * 8) = o0;
      *reinterpret_cast<f32x4*>(xs + lane * 8 + 4) = o1;
      if (lane < 8) {
#pragma unroll
        for (int k = 0; k < 4; ++k) { o0[k] = rbf(rbf(f[k] - mb) / den); o1[k] = rbf(rbf(f[4 + k] - mb) / den); }
        *reinterpret_cast<f32x4*>(xs + 512 + lane * 8) = o0;
        *reinterpret_cast<f32x4*>(xs + 512 + lane * 8 + 4) = o1;
      }
    }
    __builtin_amdgcn_s_waitcnt(0xC07F);
    __builtin_amdgcn_wave_barrier();
    {
      float fa[8];
#pragma unroll
      for (int k = 0; k < 8; ++k) fa[k] = 0.f;
#pragma unroll
      for (int i = 0; i < 9; ++i) fma8(fa, xs[frow + i], fw[i]);
      if (lane < 52) {
        float* fp = lds + L_FP + wave * P1 + 8 * lane;
        *reinterpret_cast<f32x4*>(fp) = f32x4{fa[0], fa[1], fa[2], fa[3]};
        *reinterpret_cast<f32x4*>(fp + 4) = f32x4{fa[4], fa[5], fa[6], fa[7]};
      }
    }
    __builtin_amdgcn_sched_barrier(0);
#pragma unroll
    for (int i = 0; i < 8; ++i) if (i >= PRE && i < PRE + PRE1) S[i] = ld16_nt(kp + i * 512);
#pragma unroll
    for (int i = 0; i < 8; ++i) if (8 + i >= PRE && 8 + i < PRE + PRE1) S[8 + i] = ld16_nt(vp + i * 512);
    __builtin_amdgcn_sched_barrier(0);
    __syncthreads();
    tag = reinterpret_cast<const unsigned*>(lds)[L_TAG];
    if (tid < P1 / 2) {
      float s0 = 0.f, s1 = 0.f;
#pragma unroll
      for (int w = 0; w < NWAVE; ++w) {
        const float2 v = *reinterpret_cast<const float2*>(lds + L_FP + w * P1 + 2 * tid);
        s0 += v.x; s1 += v.y;
      }
      st_gran_asm(ws + (WS_P1 + (unsigned)prod * P1_BYTES + (unsigned)tid * 16), tag, __float_as_uint(s0), __float_as_uint(s1));
    }
    __builtin_amdgcn_sched_barrier(0);
#pragma unroll
    for (int i = 0; i < 8; ++i) if (i >= PRE + PRE1) {
      asm volatile("s_waitcnt vmcnt(%0)" :: "n"(PACE - 1) : "memory");
      S[i] = ld16_nt(kp + i * 512); __builtin_amdgcn_sched_barrier(0); }
#pragma unroll
    for (int i = 0; i < 8; ++i) if (8 + i >= PRE + PRE1) {
      asm volatile("s_waitcnt vmcnt(%0)" :: "n"(PACE - 1) : "memory");
      S[8 + i] = ld16_nt(vp + i * 512); __builtin_amdgcn_sched_barrier(0); }
    __builtin_amdgcn_sched_barrier(0);
    __syncthreads();
    unsigned qpk[4];
    {
      const u32x4 t = *reinterpret_cast<const u32x4*>(lds + L_QP + 4 * c);
      qpk[0] = t[0]; qpk[1] = t[1]; qpk[2] = t[2]; qpk[3] = t[3];
    }
    float E = -INFINITY, acc[8];
    double A[8], Lsum = 0.0, fo_ = 0.0, fu_ = 0.0;
#pragma unroll
    for (int k = 0; k < 8; ++k) { acc[k] = 0.f; A[k] = 0.0; }
    const unsigned short* kn = kp;
    const unsigned short* vn = vp;
    u32x4 U[8];
    u32x4 wo0, wo1, wo2;
    {
      const unsigned short* p = wo + (size_t)(head * DH + 16 * split + 2 * wave) * DM;
      wo0 = ld16_nt(p + lane * 8); wo1 = ld16_nt(p + DM + lane * 8);
      wo2 = ld16_nt(p + ((lane >> 3) & 1) * DM + 512 + (lane & 7) * 8);
      __builtin_amdgcn_sched_barrier(0);
    }
    float p_;
#define ISSUE(P, MODE) do { constexpr int n_ = (P) + WIN;                                              \
    if constexpr ((MODE) == 2) { if constexpr ((P) < WIN) S[P] = ld16_nt(kn + (P) * 512); }            \
    else if constexpr (n_ < 8) S[n_] = ld16_nt(kp + n_ * 512);                                         \
    else if constexpr (n_ < 16) S[n_] = ld16_nt(vp + (n_ - 8) * 512);                                  \
    else if constexpr ((MODE) == 4) { if constexpr (n_ - 16 < 4) U[n_ - 16] = ld16_nt(kn + (n_ - 16) * 512); \
                                      else U[n_ - 16] = ld16_nt(vn + (n_ - 20) * 512); }               \
    else if constexpr ((MODE) != 1) S[n_ - 16] = ld16_nt(kn + (n_ - 16) * 512);                        \
    __builtin_amdgcn_sched_barrier(0); } while (0)
#define QK(P, MODE) do { float d_ = dot2(S[P][0], qpk[0], 0.f); d_ = dot2(S[P][1], qpk[1], d_);        \
    d_ = dot2(S[P][2], qpk[2], d_); xx[P] = dot2(S[P][3], qpk[3], d_);                                 \
    asm volatile("" : "+v"(xx[P]) :: "memory"); ISSUE(P, MODE); } while (0)
#define PV(I, MODE) do { asm volatile("" : "+v"(S[8 + (I)])); fma8(acc, bcast16<2 * (I)>(p_), S[8 + (I)]);                                  \
    asm volatile("" : "+v"(acc[0]), "+v"(acc[1]), "+v"(acc[2]), "+v"(acc[3]), "+v"(acc[4]), "+v"(acc[5]), "+v"(acc[6]), "+v"(acc[7]) :: "memory"); \
    ISSUE(8 + (I), MODE); } while (0)
#define PVH(I, MODE) do { if constexpr ((I) < 8 - WIN) PV(I, MODE); } while (0)
#define PVT(I, MODE) do { if constexpr ((I) >= 8 - WIN) PV(I, MODE); } while (0)
#define TR_STEP(HALF, CTRL, BIT)                                                                       \
    _Pragma("unroll") for (int h = 0; h < (HALF); ++h) {                                               \
      const float lo_ = xx[h], hi_ = xx[h + (HALF)];                                                   \
      const float t1_ = lo_ + dpp<CTRL>(lo_), t2_ = hi_ + dpp<CTRL>(hi_);                              \
      xx[h] = (c & (BIT)) ? t2_ : t1_; }
#define SOFTMAX(REP) do { const float eu_ = ceilf(mx_ * LOG2E), en_ = fmaxf(E, eu_);                   \
    p_ = __builtin_amdgcn_exp2f(fmaf(s_, LOG2E, -eu_));                                                \
    fo_ = pow2d(E, en_); fu_ = pow2d(eu_, en_); E = en_;                                               \
    Lsum = fma((double)p_, fu_ * (REP), Lsum * fo_);                                                   \
    _Pragma("unroll") for (int k = 0; k < 8; ++k) acc[k] = 0.f; } while (0)
#define FOLD() do { _Pragma("unroll") for (int k = 0; k < 8; ++k) A[k] = fma((double)acc[k], fu_, A[k] * fo_); } while (0)
#define HEAD(MODE) do {                                                                                \
    float xx[8];                                                                                       \
    QK(0, MODE); QK(1, MODE); QK(2, MODE); QK(3, MODE); QK(4, MODE); QK(5, MODE); QK(6, MODE); QK(7, MODE); \
             \
    TR_STEP(4, ROR8, 8) TR_STEP(2, XOR7, 4) TR_STEP(1, XOR2, 2)                                        \
    const float sd_ = xx[0] + dpp<XOR1>(xx[0]);                                                        \
    const float s_ = rbf(rbf(sd_) * SCALE);                                                            \
    float mx_ = fmaxf(s_, dpp<ROR8>(s_)); mx_ = fmaxf(mx_, dpp<ROR4>(mx_)); mx_ = fmaxf(mx_, dpp<ROR2>(mx_)); \
    SOFTMAX(0.5);                                                                                      \
    PVH(0, MODE); PVH(1, MODE); PVH(2, MODE); PVH(3, MODE); PVH(4, MODE); PVH(5, MODE); PVH(6, MODE); PVH(7, MODE); \
  } while (0)
#define TAIL(MODE) do {                                                                                \
    PVT(0, MODE); PVT(1, MODE); PVT(2, MODE); PVT(3, MODE); PVT(4, MODE); PVT(5, MODE); PVT(6, MODE); PVT(7, MODE); \
    FOLD(); } while (0)
    unsigned slot = 0;
#define REQUEST() do { if (lane == 0) slot = __hip_atomic_fetch_add(reinterpret_cast<unsigned*>(lds) + L_REQ, 1u, __ATOMIC_RELAXED, __HIP_MEMORY_SCOPE_WORKGROUP); \
    slot = __builtin_amdgcn_readfirstlane(slot); } while (0)
#define TAKE(T) do { unsigned spins_ = 0;                                                              \
    for (;;) { T = __builtin_amdgcn_readfirstlane(__hip_atomic_load(reinterpret_cast<unsigned*>(lds) + L_TQ + slot, __ATOMIC_RELAXED, __HIP_MEMORY_SCOPE_WORKGROUP)); \
      if (T != TQ_EMPTY || ++spins_ > (1u << 20)) break;                                               \
      __builtin_amdgcn_s_sleep(1); } } while (0)
#define UNIT_OFF(T) ((size_t)(((T) & 7u) * (unsigned)(SK / NSPLIT) + (unsigned)(NST * NWAVE * 32) + ((T) >> 3) * 16u) * DH)
#define UISSUE(P, MODE) do { constexpr int n_ = (P) + WIN;                                             \
    if constexpr (n_ < 4) U[n_] = ld16_nt(kp + n_ * 512);                                              \
    else if constexpr (n_ < 8) U[n_] = ld16_nt(vp + (n_ - 4) * 512);                                   \
    else if constexpr ((MODE) == 0) { if constexpr (n_ - 8 < 4) U[n_ - 8] = ld16_nt(kn + (n_ - 8) * 512); \
                                      else U[n_ - 8] = ld16_nt(vn + (n_ - 12) * 512); }                \
    __builtin_amdgcn_sched_barrier(0); } while (0)
#define USTEP(P, MODE) do {                                                                            \
    if constexpr ((P) < 4) {                                                                           \
      float d_ = dot2(U[P][0], qpk[0], 0.f); d_ = dot2(U[P][1], qpk[1], d_);                           \
      d_ = dot2(U[P][2], qpk[2], d_); xx[(P) & 3] = dot2(U[P][3], qpk[3], d_);                         \
      asm volatile("" : "+v"(xx[(P) & 3]) :: "memory"); UISSUE(P, MODE);                               \
      if constexpr ((P) == 3) {                                                                        \
                          \
        TR_STEP(2, ROR8, 8) TR_STEP(1, XOR7, 4)                                                        \
        float sd_ = xx[0] + dpp<XOR2>(xx[0]); sd_ += dpp<XOR1>(sd_);                                   \
        const float s_ = rbf(rbf(sd_) * SCALE);                                                        \
        float mx_ = fmaxf(s_, dpp<ROR8>(s_)); mx_ = fmaxf(mx_, dpp<ROR4>(mx_));                        \
        SOFTMAX(0.25);                                                                                 \
      }                                                                                                \
    } else {                                                                                           \
      fma8(acc, bcast16<4 * ((P) & 3)>(p_), U[P]);                                                     \
      asm volatile("" : "+v"(acc[0]), "+v"(acc[1]), "+v"(acc[2]), "+v"(acc[3]), "+v"(acc[4]), "+v"(acc[5]), "+v"(acc[6]), "+v"(acc[7]) :: "memory"); \
      UISSUE(P, MODE);                                                                                 \
    } } while (0)
#define UHEAD() do { if constexpr (0 < 8 - WIN) USTEP(0, 1); if constexpr (1 < 8 - WIN) USTEP(1, 1); if constexpr (2 < 8 - WIN) USTEP(2, 1); \
    if constexpr (3 < 8 - WIN) USTEP(3, 1); if constexpr (4 < 8 - WIN) USTEP(4, 1); if constexpr (5 < 8 - WIN) USTEP(5, 1); if constexpr (6 < 8 - WIN) USTEP(6, 1); } while (0)
#define UTAIL(MODE) do { if constexpr (0 >= 8 - WIN) USTEP(0, MODE); if constexpr (1 >= 8 - WIN) USTEP(1, MODE); if constexpr (2 >= 8 - WIN) USTEP(2, MODE); \
    if constexpr (3 >= 8 - WIN) USTEP(3, MODE); if constexpr (4 >= 8 - WIN) USTEP(4, MODE); if constexpr (5 >= 8 - WIN) USTEP(5, MODE); \
    if constexpr (6 >= 8 - WIN) USTEP(6, MODE); USTEP(7, MODE); FOLD(); } while (0)
    static_assert(NST >= 3, "the first three chunks are static");
    unsigned nx = chunk0 + NWAVE;
    kn = kc + hoff + (size_t)nx * (32 * DH); vn = vc + hoff + (size_t)nx * (32 * DH);
    HEAD(2);
    TAIL(2);
    kp = kn; vp = vn;
    HEAD(0);
    constexpr int NSU = 2 * (NST - 2);
    const unsigned ubase = (unsigned)(split * (SK / NSPLIT) + 2 * NWAVE * 32 + wave * 16);
    {
      kn = kc + hoff + (size_t)ubase * DH; vn = vc + hoff + (size_t)ubase * DH;
      TAIL(4);
      kp = kn; vp = vn;
      float xx[4];
      UHEAD();
      int u = 1;
      size_t noff = (size_t)(ubase + 128u) * DH;
      bool more = true;
#pragma unroll 1
      do {
        if (u + 1 >= NSU) REQUEST();
        kn = kc + hoff + noff; vn = vc + hoff + noff;
        UTAIL(0);
        kp = kn; vp = vn;
        UHEAD();
        ++u;
        if (u >= NSU) {
          unsigned tn;
          TAKE(tn);
          more = tn < (unsigned)NDYN;
          noff = UNIT_OFF(tn);
        } else {
          noff += (size_t)128 * DH;
        }
      } while (more);
      UTAIL(1);
    }
#undef UTAIL
#undef UHEAD
#undef USTEP
#undef UISSUE
#undef UNIT_OFF
#undef TAKE
#undef REQUEST
#undef TAIL
#undef HEAD
#undef FOLD
#undef SOFTMAX
#undef TR_STEP
#undef PVT
#undef PVH
#undef PV
#undef QK
#undef ISSUE
    {
      const float ew = x32_max(x16_max(E));
      const double fs = pow2d(E, ew);
      double lw = Lsum * fs;
      lw += dppd<ROR8>(lw); lw += dppd<ROR4>(lw); lw += dppd<ROR2>(lw); lw += dppd<ROR1>(lw);
      lw = x32_sumd(x16_sumd(lw));
#pragma unroll
      for (int k = 0; k < 8; ++k) A[k] *= fs;
      double B[4], C[2];
#pragma unroll
      for (int k = 0; k < 4; ++k) {
        auto lo = __builtin_amdgcn_permlane32_swap(dlo(A[k]), dlo(A[k + 4]), false, false);
        auto hi = __builtin_amdgcn_permlane32_swap(dhi(A[k]), dhi(A[k + 4]), false, false);
        B[k] = mkd(lo[0], hi[0]) + mkd(lo[1], hi[1]);
      }
#pragma unroll
      for (int k = 0; k < 2; ++k) {
        auto lo = __builtin_amdgcn_permlane16_swap(dlo(B[k]), dlo(B[k + 2]), false, false);
        auto hi = __builtin_amdgcn_permlane16_swap(dhi(B[k]), dhi(B[k + 2]), false, false);
        C[k] = mkd(lo[0], hi[0]) + mkd(lo[1], hi[1]);
      }
      float* mg = lds + L_MG + wave * 260;
      double* rec = reinterpret_cast<double*>(mg + 2);
      rec[1 + 8 * c + 2 * r] = C[0]; rec[2 + 8 * c + 2 * r] = C[1];
      if (lane == 0) { mg[0] = ew; rec[0] = lw; }
    }
    __syncthreads();
    if (idx >= 23) xv = __uint_as_float((unsigned)x[(xcd * 9 + idx - 23) * 8 + (tid & 7)] << 16);
    if (idx >= 23)
      gather_part(lds + L_RD, rs, tag, WS_YF + (unsigned)(xcd * 9 + idx - 23) * (NWG * 64) + (unsigned)tid * 32, lane, wave, 1);
    __syncthreads();
    {
      const float2 ov = *reinterpret_cast<const float2*>(lds + L_OV + 2 * wave);
      float y[8], y2[8];
#pragma unroll
      for (int k = 0; k < 8; ++k) { y[k] = 0.f; y2[k] = 0.f; }
      fma8(y, ov.x, wo0); fma8(y, ov.y, wo1);
      fma8(y2, (lane & 8) ? ov.y : ov.x, wo2);
#pragma unroll
      for (int k = 0; k < 8; ++k) y2[k] += dpp<ROR8>(y2[k]);
      float* yp = lds + L_YP + wave * DM;
      *reinterpret_cast<f32x4*>(yp + lane * 8) = f32x4{y[0], y[1], y[2], y[3]};
      *reinterpret_cast<f32x4*>(yp + lane * 8 + 4) = f32x4{y[4], y[5], y[6], y[7]};
      if (lane < 8) {
        *reinterpret_cast<f32x4*>(yp + 512 + lane * 8) = f32x4{y2[0], y2[1], y2[2], y2[3]};
        *reinterpret_cast<f32x4*>(yp + 512 + lane * 8 + 4) = f32x4{y2[4], y2[5], y2[6], y2[7]};
      }
    }
    __syncthreads();
    if (tid < 288) {
      float s0 = 0.f, s1 = 0.f;
#pragma unroll
      for (int w = 0; w < NWAVE; ++w) {
        const float2 t = *reinterpret_cast<const float2*>(lds + L_YP + w * DM + 2 * tid);
        s0 += t.x; s1 += t.y;
      }
      st_gran(rs, WS_YA + (unsigned)(tid >> 2) * (NWG * 64) + (unsigned)prod * 64 + (unsigned)(tid & 3) * 16, tag, s0, s1);
    }
  }
  if (idx >= 23) {
    const int g = xcd * 9 + (idx - 23);
    gather_part(lds + L_RD + 64, rs, tag, WS_YA + (unsigned)g * (NWG * 64) + (unsigned)tid * 32, lane, wave, 0);
    __syncthreads();
    if (tid < 8) {
      float sa = 0.f, sf = 0.f;
#pragma unroll
      for (int w = 0; w < NWAVE; ++w) { sf += lds[L_RD + w * 8 + tid]; sa += lds[L_RD + 64 + w * 8 + tid]; }
      const int col = g * 8 + tid;
      float rr = rbf(xv + rbf(sa));
      rr = rr + rbf(sf);
      out[col] = __builtin_bit_cast(unsigned short, (__bf16)rr);
    }
    if (g == 0 && tid == 0) __builtin_amdgcn_raw_buffer_store_b32(tag, rs, (int)WS_EPOCH, 0, AUX_SC1);
  }
}

}

extern "C" void kernel_launch(void* const* d_in, const int* in_sizes, int n_in,
                              void* d_out, int out_size, void* d_ws, size_t ws_size,
                              hipStream_t stream) {
  const unsigned short* x = (const unsigned short*)d_in[0];
  const unsigned short* kc = (const unsigned short*)d_in[1];
  const unsigned short* vc = (const unsigned short*)d_in[2];
  const unsigned short* wq = (const unsigned short*)d_in[3];
  const unsigned short* wo = (const unsigned short*)d_in[4];
  const unsigned short* wg = (const unsigned short*)d_in[6];
  const unsigned short* wu = (const unsigned short*)d_in[7];
  const unsigned short* wd = (const unsigned short*)d_in[8];
  fused_decode_kernel<<<NWG, NTHR_ALL, 0, stream>>>(x, kc, vc, wq, wo, wg, wu, wd, (unsigned short*)d_out, (unsigned char*)d_ws);
}
```
